```python
import torch

def _softmax(x, axis=-1):
    return torch.softmax(x, dim=axis)
def _gelu(x):
    return torch.nn.functional.gelu(x, approximate="tanh")
def _silu(x):
    return torch.nn.functional.silu(x)
def _sigmoid(x):
    return torch.sigmoid(x)
def _layernorm(x, axis=-1, eps=1e-5):
    m = x.mean(dim=axis, keepdim=True)
    v = x.var(dim=axis, keepdim=True, unbiased=False)
    return (x - m) / torch.sqrt(v + eps)
def _scatter_add(tgt, idx, src, axis=0):
    return tgt.clone().index_add_(axis, idx, src)
def _segment_sum(src, seg, n):
    out = torch.zeros((n,) + src.shape[1:], dtype=src.dtype)
    return out.index_add_(0, seg, src)
def _pad(x, pw):
    flat = tuple(v for pair in reversed(pw) for v in pair)
    return torch.nn.functional.pad(x, flat)

kernel_name = 'recurrent_scan__mha_block_cached__glu_gated_geglu__decode'

def setup_inputs():

    _w = (lambda a: (lambda t: t.to(torch.bfloat16) if t.is_floating_point() else t)(torch.as_tensor(a)))
    def _A_setup():
        B, Sq, D = 2, 1, 640
        H, Sk, Dh = 32, 16384, 128
        return {
            "x": _w((lambda *s: torch.randn(*s, dtype=torch.bfloat16) * 0.4)(B, Sq, D)),
            "k_cache": _w((lambda *s: torch.randn(*s, dtype=torch.bfloat16) * 1.6)(B, H, Sk, Dh)),
            "v_cache": _w((lambda *s: torch.randn(*s, dtype=torch.bfloat16) * 1.6)(B, H, Sk, Dh)),
            "wq": _w((lambda *s: torch.randn(*s, dtype=torch.bfloat16) * 1.6)(D, H * Dh) * (D ** -0.5)),
            "wo": _w((lambda *s: torch.randn(*s, dtype=torch.bfloat16) * 1.6)(H * Dh, D) * ((H * Dh) ** -0.5)),
        }
    _A_in = _A_setup()

    def _B_setup():
        B, S, Dm, Df = 2, 1, 640, 1728
        return {
            "x": _w((lambda *s: torch.randn(*s, dtype=torch.bfloat16) * 0.4)(B, S, Dm)),
            "w_gate": _w((lambda *s: torch.randn(*s, dtype=torch.bfloat16) * 1.6)(Dm, Df) * (Dm ** -0.5)),
            "w_up": _w((lambda *s: torch.randn(*s, dtype=torch.bfloat16) * 1.6)(Dm, Df) * (Dm ** -0.5)),
            "w_down": _w((lambda *s: torch.randn(*s, dtype=torch.bfloat16) * 1.6)(Df, Dm) * (Df ** -0.5)),
        }
    _B_in = _B_setup()

    return {
        "A_x": _A_in["x"],
        "A_k_cache": _A_in["k_cache"],
        "A_v_cache": _A_in["v_cache"],
        "A_wq": _A_in["wq"],
        "A_wo": _A_in["wo"],
        "B_x": _B_in["x"],
        "B_w_gate": _B_in["w_gate"],
        "B_w_up": _B_in["w_up"],
        "B_w_down": _B_in["w_down"],
    }

def reference(A_x, A_k_cache, A_v_cache, A_wq, A_wo, B_x, B_w_gate, B_w_up, B_w_down):
    def _A_ref(x, k_cache, v_cache, wq, wo):
        B, Sq, D = x.shape
        H, Dh = 32, 128
        q = (lambda x, *d: x.permute(*d))((x @ wq).reshape(B, Sq, H, Dh), 0, 2, 1, 3)
        scores = torch.einsum("bhqd,bhkd->bhqk", q, k_cache) * (Dh ** -0.5)
        p = _softmax(scores, axis=-1)
        o = torch.einsum("bhqk,bhkd->bhqd", p, v_cache)
        return (lambda x, *d: x.permute(*d))(o, 0, 2, 1, 3).reshape(B, Sq, H * Dh) @ wo

    def _B_ref(x, w_gate, w_up, w_down):
        g = torch.einsum("bsd,df->bsf", x, w_gate)
        u = torch.einsum("bsd,df->bsf", x, w_up)
        h = _gelu(g) * u
        return torch.einsum("bsf,fd->bsd", h, w_down)

    x = A_x
    xn = _layernorm(x, axis=-1)
    s = _A_ref(xn, A_k_cache, A_v_cache, A_wq, A_wo)
    g = _silu(_B_ref(xn, B_w_gate, B_w_up, B_w_down))
    return x + s * g
```

```cpp
#include <hip/hip_runtime.h>
#include <hip/hip_bf16.h>
#include <stdint.h>

typedef unsigned int u32;
typedef u32 u32x4 __attribute__((ext_vector_type(4)));
typedef float f32x4 __attribute__((ext_vector_type(4)));
typedef __bf16 bf16x2 __attribute__((ext_vector_type(2)));
typedef unsigned short u16;

constexpr int NB = 2, DM = 640, NH = 32, SK = 16384, DH = 128, DF = 1728;
constexpr int NSPLIT = 4;

__device__ __forceinline__ float bf2f(u16 v) { return __builtin_bit_cast(float, (u32)v << 16); }
__device__ __forceinline__ float bflo(u32 v) { return __builtin_bit_cast(float, v << 16); }
__device__ __forceinline__ float bfhi(u32 v) { return __builtin_bit_cast(float, v & 0xffff0000u); }
__device__ __forceinline__ u16 f2bf(float f) { __bf16 b = (__bf16)f; return __builtin_bit_cast(u16, b); }
__device__ __forceinline__ float rbf(float f) { return bf2f(f2bf(f)); }
__device__ __forceinline__ float dot2(u32 k, u32 q, float acc) {
  return __builtin_amdgcn_fdot2_f32_bf16(__builtin_bit_cast(bf16x2, k), __builtin_bit_cast(bf16x2, q), acc, false);
}
template <int CTRL> __device__ __forceinline__ float dpp(float x) {
  return __builtin_bit_cast(float, __builtin_amdgcn_mov_dpp(__builtin_bit_cast(int, x), CTRL, 0xf, 0xf, true));
}
constexpr int XOR1 = 0xB1, XOR2 = 0x4E, XOR7 = 0x141, XOR8 = 0x128, ROR4 = 0x124, ROR2 = 0x122, ROR1 = 0x121;

__device__ __forceinline__ void layernorm_lds(const u16* x, float* xn, float* red) {
  const int tid = threadIdx.x, nt = blockDim.x;
  for (int b = 0; b < NB; ++b) {
    float s = 0.f;
    for (int i = tid; i < DM; i += nt) s += bf2f(x[b * DM + i]);
    for (int o = 32; o; o >>= 1) s += __shfl_xor(s, o);
    if ((tid & 63) == 0) red[tid >> 6] = s;
    __syncthreads();
    float t = 0.f; for (int w = 0; w < nt / 64; ++w) t += red[w];
    __syncthreads();
    const float mean = t / DM;
    float v = 0.f;
    for (int i = tid; i < DM; i += nt) { float d = bf2f(x[b * DM + i]) - mean; v += d * d; }
    for (int o = 32; o; o >>= 1) v += __shfl_xor(v, o);
    if ((tid & 63) == 0) red[tid >> 6] = v;
    __syncthreads();
    float tv = 0.f; for (int w = 0; w < nt / 64; ++w) tv += red[w];
    __syncthreads();
    const float var = rbf(tv / DM), mb = rbf(mean);
    const float den = rbf(sqrtf(rbf(var + 1e-5f)));
    for (int i = tid; i < DM; i += nt) xn[b * DM + i] = rbf(rbf(bf2f(x[b * DM + i]) - mb) / den);
  }
  __syncthreads();
}

__global__ __launch_bounds__(256) void k_pre(const u16* x, const u16* wq, const u16* wg, const u16* wu, u16* q, float* hh) {
  __shared__ float xn[NB * DM];
  __shared__ float red[16];
  __shared__ float part[4][4][64];
  layernorm_lds(x, xn, red);
  const int tid = threadIdx.x, col = tid & 63, ks = tid >> 6;
  if (blockIdx.x < 64) {
    const int c0 = blockIdx.x * 64 + col;
    float a0 = 0, a1 = 0;
    for (int k = ks * 160; k < ks * 160 + 160; ++k) { float w = bf2f(wq[(size_t)k * (NH * DH) + c0]); a0 += xn[k] * w; a1 += xn[DM + k] * w; }
    part[0][ks][col] = a0; part[1][ks][col] = a1;
    __syncthreads();
    if (tid < 128) { int b = tid >> 6; float s = part[b][0][col] + part[b][1][col] + part[b][2][col] + part[b][3][col]; q[b * NH * DH + c0] = f2bf(s); }
  } else {
    const int c0 = (blockIdx.x - 64) * 64 + col;
    float g0 = 0, g1 = 0, u0 = 0, u1 = 0;
    for (int k = ks * 160; k < ks * 160 + 160; ++k) {
      float w = bf2f(wg[(size_t)k * DF + c0]), w2 = bf2f(wu[(size_t)k * DF + c0]);
      g0 += xn[k] * w; g1 += xn[DM + k] * w; u0 += xn[k] * w2; u1 += xn[DM + k] * w2; }
    part[0][ks][col] = g0; part[1][ks][col] = g1; part[2][ks][col] = u0; part[3][ks][col] = u1;
    __syncthreads();
    if (tid < 128) { int b = tid >> 6;
      float g = rbf(part[b][0][col] + part[b][1][col] + part[b][2][col] + part[b][3][col]);
      float u = rbf(part[2 + b][0][col] + part[2 + b][1][col] + part[2 + b][2][col] + part[2 + b][3][col]);
      float t = tanhf(0.7978845608028654f * (g + 0.044715f * g * g * g));
      float ge = rbf(0.5f * g * (1.f + t));
      hh[b * DF + c0] = rbf(ge * u); }
  }
}

__global__ __launch_bounds__(512, 2) void k_attn(const u16* __restrict__ kc, const u16* __restrict__ vc, const u16* __restrict__ q, float* __restrict__ po, float* __restrict__ pml) {
  __shared__ float mrg[8 * 4][132];
  const int blk = blockIdx.x;
  const int xcd = blk & 7, j = blk >> 3;
  const int h = xcd * 4 + (j >> 3), b = (j >> 2) & 1, sp = j & 3;
  const int tid = threadIdx.x, lane = tid & 63, wave = __builtin_amdgcn_readfirstlane(tid >> 6), r = lane >> 4, c = lane & 15;
  const size_t base = ((size_t)(b * NH + h) * SK + (size_t)sp * (SK / NSPLIT)) * DH;
  const u32x4* kp = (const u32x4*)(kc + base) + wave * 1024 + lane;
  const u32x4* vp = (const u32x4*)(vc + base) + wave * 1024 + lane;
  constexpr int NT = SK / NSPLIT / 64 / 8;
  u32x4 K[16], V[16];
#pragma unroll
  for (int i = 0; i < 16; ++i) K[i] = __builtin_nontemporal_load(kp + i * 64);
#pragma unroll
  for (int i = 0; i < 16; ++i) V[i] = __builtin_nontemporal_load(vp + i * 64);
  __builtin_amdgcn_sched_barrier(0);
  const u32x4 qv = *(const u32x4*)(q + (size_t)(b * NH + h) * DH + 8 * c);
  float m = -INFINITY, l = 0.f, acc[8];
#pragma unroll
  for (int k = 0; k < 8; ++k) acc[k] = 0.f;
  constexpr float C2 = 0.088388347648318440f * 1.4426950408889634f;
  for (int t = 0; t < NT; ++t) {
    const bool more = t + 1 < NT;
    const u32x4* kn = kp + (more ? (t + 1) * 8192 : 0);
    const u32x4* vn = vp + (more ? (t + 1) * 8192 : 0);
    float x[16];
#pragma unroll
    for (int i = 0; i < 16; ++i) {
      float d = dot2(K[i][0], qv[0], 0.f); d = dot2(K[i][1], qv[1], d); d = dot2(K[i][2], qv[2], d); d = dot2(K[i][3], qv[3], d);
      x[i] = d;
      if (more) K[i] = __builtin_nontemporal_load(kn + i * 64);
      __builtin_amdgcn_sched_barrier(0);
    }
#define TR_STEP(HALF, CTRL, BIT) _Pragma("unroll") for (int hh = 0; hh < (HALF); ++hh) { \
      const float lo_ = x[hh], hi_ = x[hh + (HALF)]; const float t1_ = lo_ + dpp<CTRL>(lo_), t2_ = hi_ + dpp<CTRL>(hi_); \
      x[hh] = (c & (BIT)) ? t2_ : t1_; }
    TR_STEP(8, XOR8, 8) TR_STEP(4, XOR7, 4) TR_STEP(2, XOR2, 2) TR_STEP(1, XOR1, 1)
#undef TR_STEP
    const float sc = x[0] * C2;
    float pm = sc;
    pm = fmaxf(pm, dpp<XOR8>(pm)); pm = fmaxf(pm, dpp<ROR4>(pm)); pm = fmaxf(pm, dpp<ROR2>(pm)); pm = fmaxf(pm, dpp<ROR1>(pm));
    const float mn = fmaxf(m, pm);
    const float alpha = __builtin_amdgcn_exp2f(m - mn), p = __builtin_amdgcn_exp2f(sc - mn);
    l = fmaf(l, alpha, p); m = mn;
#pragma unroll
    for (int k = 0; k < 8; ++k) acc[k] *= alpha;
#define PV(i) { const float pb = __builtin_bit_cast(float, __builtin_amdgcn_mov_dpp(__builtin_bit_cast(int, p), 0x150 + (i), 0xf, 0xf, true)); \
      _Pragma("unroll") for (int k = 0; k < 4; ++k) { acc[2 * k] = fmaf(pb, bflo(V[i][k]), acc[2 * k]); acc[2 * k + 1] = fmaf(pb, bfhi(V[i][k]), acc[2 * k + 1]); } \
      if (more) V[i] = __builtin_nontemporal_load(vn + (i) * 64); __builtin_amdgcn_sched_barrier(0); }
    PV(0) PV(1) PV(2) PV(3) PV(4) PV(5) PV(6) PV(7) PV(8) PV(9) PV(10) PV(11) PV(12) PV(13) PV(14) PV(15)
#undef PV
  }
  l += dpp<XOR8>(l); l += dpp<ROR4>(l); l += dpp<ROR2>(l); l += dpp<ROR1>(l);
  float* rec = mrg[wave * 4 + r];
  *(f32x4*)(rec + 4 + 8 * c) = f32x4{acc[0], acc[1], acc[2], acc[3]};
  *(f32x4*)(rec + 8 + 8 * c) = f32x4{acc[4], acc[5], acc[6], acc[7]};
  if (c == 0) { rec[0] = m; rec[1] = l; }
  __syncthreads();
  if (tid < 128) {
    float M = -INFINITY;
    for (int w = 0; w < 32; ++w) M = fmaxf(M, mrg[w][0]);
    float L = 0.f, o = 0.f;
    for (int w = 0; w < 32; ++w) { const float wt = __builtin_amdgcn_exp2f(mrg[w][0] - M); L = fmaf(wt, mrg[w][1], L); o = fmaf(wt, mrg[w][4 + tid], o); }
    const int slot = (b * NH + h) * NSPLIT + sp;
    po[slot * DH + tid] = o;
    if (tid == 0) { pml[slot * 2] = M; pml[slot * 2 + 1] = L; }
  }
}

__global__ __launch_bounds__(256) void k_post(const u16* x, const u16* wo, const u16* wd, const float* po, const float* pml, const float* hh, u16* out) {
  __shared__ float o[NB][NH * DH];
  __shared__ float hs[NB][DF];
  __shared__ float part[8][4][32];
  const int tid = threadIdx.x;
  for (int i = tid; i < NB * NH * DH; i += 256) {
    const int bh = i >> 7, d = i & 127;
    float M = -INFINITY;
    for (int s = 0; s < NSPLIT; ++s) M = fmaxf(M, pml[(bh * NSPLIT + s) * 2]);
    float L = 0.f, a = 0.f;
    for (int s = 0; s < NSPLIT; ++s) { const float w = __builtin_amdgcn_exp2f(pml[(bh * NSPLIT + s) * 2] - M); L = fmaf(w, pml[(bh * NSPLIT + s) * 2 + 1], L); a = fmaf(w, po[(bh * NSPLIT + s) * DH + d], a); }
    (&o[0][0])[i] = rbf(a / L);
  }
  for (int i = tid; i < NB * DF; i += 256) (&hs[0][0])[i] = hh[i];
  __syncthreads();
  const int col = blockIdx.x * 32 + (tid & 31), ks = tid >> 5;
  float s0 = 0, s1 = 0, y0 = 0, y1 = 0;
  for (int k = ks * 512; k < ks * 512 + 512; ++k) { float w = bf2f(wo[(size_t)k * DM + col]); s0 += o[0][k] * w; s1 += o[1][k] * w; }
  for (int k = ks * 216; k < ks * 216 + 216; ++k) { float w = bf2f(wd[(size_t)k * DM + col]); y0 += hs[0][k] * w; y1 += hs[1][k] * w; }
  part[ks][0][tid & 31] = s0; part[ks][1][tid & 31] = s1; part[ks][2][tid & 31] = y0; part[ks][3][tid & 31] = y1;
  __syncthreads();
  if (tid < 64) {
    const int b = tid >> 5, cc = tid & 31;
    float s = 0, y = 0;
    for (int w = 0; w < 8; ++w) { s += part[w][b][cc]; y += part[w][2 + b][cc]; }
    s = rbf(s); y = rbf(y);
    const float g = rbf(y / (1.f + __expf(-y)));
    const int oc = blockIdx.x * 32 + cc;
    out[b * DM + oc] = f2bf(bf2f(x[b * DM + oc]) + rbf(s * g));
  }
}

extern "C" void kernel_launch(void* const* d_in, const int* in_sizes, int n_in, void* d_out, int out_size, void* d_ws, size_t ws_size, hipStream_t stream) {
  const u16* x = (const u16*)d_in[0];
  const u16* kc = (const u16*)d_in[1];
  const u16* vc = (const u16*)d_in[2];
  const u16* wq = (const u16*)d_in[3];
  const u16* wo = (const u16*)d_in[4];
  const u16* wg = (const u16*)d_in[6];
  const u16* wu = (const u16*)d_in[7];
  const u16* wd = (const u16*)d_in[8];
  char* ws = (char*)d_ws;
  u16* q = (u16*)ws;
  float* hh = (float*)(ws + 16384);
  float* pml = (float*)(ws + 32768);
  float* po = (float*)(ws + 65536);
  k_pre<<<64 + 27, 256, 0, stream>>>(x, wq, wg, wu, q, hh);
  k_attn<<<256, 512, 0, stream>>>(kc, vc, q, po, pml);
  k_post<<<20, 256, 0, stream>>>(x, wo, wd, po, pml, hh, (u16*)d_out);
}
```

```cpp
#include <hip/hip_runtime.h>
#include <hip/hip_bf16.h>
#include <stdint.h>

typedef unsigned int u32;
typedef unsigned long long u64;
typedef unsigned short u16;
typedef u32 u32x4 __attribute__((ext_vector_type(4)));
typedef u32 u32x2 __attribute__((ext_vector_type(2)));
typedef float f32x4 __attribute__((ext_vector_type(4)));
typedef float f32x2 __attribute__((ext_vector_type(2)));
typedef short s16x4 __attribute__((ext_vector_type(4)));
typedef short s16x8 __attribute__((ext_vector_type(8)));
typedef __bf16 bf16x2 __attribute__((ext_vector_type(2)));
typedef __bf16 bf16x8 __attribute__((ext_vector_type(8)));
#define GAS __attribute__((address_space(1)))
#define LAS __attribute__((address_space(3)))
typedef GAS u32 gu32;
typedef GAS u64 gu64;
#define RLX_AGENT __ATOMIC_RELAXED, __HIP_MEMORY_SCOPE_AGENT

#define NS_STATIC 56
#define STAMPS 0
#define DOZE 0
#define PRESLEEP 0
#define NPRE 4

constexpr int DM = 640, NH = 32, SK = 16384, DH = 128, DF = 1728;
constexpr int NW = 4;
constexpr int NTHR = (NW + 1) * 64;
constexpr int TL = 4;
constexpr int TILE_B = TL * 1024;
constexpr int SLAB_B = SK * DH * 2;
constexpr int NTILES = SLAB_B / TILE_B;
constexpr int NS = NS_STATIC;
constexpr int POOL0 = 4 * NW * NS;
constexpr int NCB = DF / 64;
constexpr u32 SPIN_CAP = 1u << 16;
constexpr int AUX_NT = 2, AUX_SC1 = 16;
static_assert(POOL0 <= NTILES && NS >= NPRE + 2, "static part");

constexpr int WS_EPOCH = 0;
constexpr int WS_CNT = 4096;
constexpr int WS_QP = 32768;
constexpr int WS_GP = WS_QP + NH * 8 * 256 * 8;
constexpr int WS_YP = WS_GP + NCB * 8 * 256 * 8;
constexpr int WS_OP = WS_YP + 8 * NCB * 160 * 8;
constexpr int WS_SP = WS_OP + NH * 8 * 256 * 8;
constexpr int WS_ST = WS_SP + 8 * NH * 160 * 8;
constexpr int WS_END = WS_ST + 256 * 16 * 8;

constexpr int L_WO = 0;
constexpr int L_WD = L_WO + 128 * 160;
constexpr int L_QBF = L_WD + 64 * 160;
constexpr int L_XN = L_QBF + 256;
constexpr int L_HS = L_XN + 640;
constexpr int L_GATE = L_HS + 512;
constexpr int L_MRG = L_GATE + 64;
constexpr int L_OF = L_MRG + NW * 528;
constexpr int L_USED = L_OF + 512;
constexpr int L_END = 84 * 1024;
static_assert(L_USED <= L_END, "LDS");

__device__ __forceinline__ float bf2f(u16 v) { return __builtin_bit_cast(float, (u32)v << 16); }
__device__ __forceinline__ float bflo(u32 v) { return __builtin_bit_cast(float, v << 16); }
__device__ __forceinline__ float bfhi(u32 v) { return __builtin_bit_cast(float, v & 0xffff0000u); }
__device__ __forceinline__ u16 f2bf(float f) { __bf16 b = (__bf16)f; return __builtin_bit_cast(u16, b); }
__device__ __forceinline__ u32 pack2(float lo, float hi) { return (u32)f2bf(lo) | ((u32)f2bf(hi) << 16); }
__device__ __forceinline__ float rbf(float f) { return bf2f(f2bf(f)); }
__device__ __forceinline__ u32 fbits(float f) { return __builtin_bit_cast(u32, f); }
__device__ __forceinline__ float bitsf(u32 u) { return __builtin_bit_cast(float, u); }
__device__ __forceinline__ float dot2(u32 k, u32 q, float acc) {
  return __builtin_amdgcn_fdot2_f32_bf16(__builtin_bit_cast(bf16x2, k), __builtin_bit_cast(bf16x2, q), acc, false);
}
template <int CTRL> __device__ __forceinline__ float dpp(float x) {
  return __builtin_bit_cast(float, __builtin_amdgcn_mov_dpp(__builtin_bit_cast(int, x), CTRL, 0xf, 0xf, true));
}
constexpr int XOR1 = 0xB1, XOR2 = 0x4E, XOR7 = 0x141, XOR8 = 0x128, ROR4 = 0x124, ROR2 = 0x122, ROR1 = 0x121;
__device__ __forceinline__ float fold16(float a, float b) { auto s = __builtin_amdgcn_permlane16_swap(fbits(a), fbits(b), false, false); return bitsf(s[0]) + bitsf(s[1]); }
__device__ __forceinline__ float fold32(float a, float b) { auto s = __builtin_amdgcn_permlane32_swap(fbits(a), fbits(b), false, false); return bitsf(s[0]) + bitsf(s[1]); }
__device__ __forceinline__ float x16_sum(float x) { return fold16(x, x); }
__device__ __forceinline__ float x32_sum(float x) { return fold32(x, x); }
__device__ __forceinline__ float x16_max(float x) { auto s = __builtin_amdgcn_permlane16_swap(fbits(x), fbits(x), false, false); return fmaxf(bitsf(s[0]), bitsf(s[1])); }
__device__ __forceinline__ float x32_max(float x) { auto s = __builtin_amdgcn_permlane32_swap(fbits(x), fbits(x), false, false); return fmaxf(bitsf(s[0]), bitsf(s[1])); }
__device__ __forceinline__ float row_sum(float x) { x += dpp<XOR8>(x); x += dpp<ROR4>(x); x += dpp<ROR2>(x); x += dpp<ROR1>(x); return x; }
__device__ __forceinline__ float wave_sum(float x) { return x32_sum(x16_sum(row_sum(x))); }
__device__ __forceinline__ void put(gu64* g, u32 tag, float v) { __hip_atomic_store(g, ((u64)tag << 32) | fbits(v), RLX_AGENT); }
__device__ __forceinline__ u64 get(gu64* g) { return __hip_atomic_load(g, RLX_AGENT); }
#define LDS_BARRIER() asm volatile("s_waitcnt lgkmcnt(0)\n\ts_barrier" ::: "memory")
#define SB() __builtin_amdgcn_sched_barrier(0)
template <int N> __device__ __forceinline__ void sweep(gu64* g, int stride, u32 tag, float (&v)[N], bool active, bool doze) {
  for (u32 spins = 0; spins < SPIN_CAP; ++spins) {
    bool ok = true;
    if (active) {
#pragma unroll
      for (int k = 0; k < N; ++k) { const u64 x = get(g + k * stride); v[k] = bitsf((u32)x); ok &= (u32)(x >> 32) == tag; }
    }
    if (__all(ok)) return;
    if (doze) { if (DOZE == 0) __builtin_amdgcn_s_sleep(8); _Pragma("unroll") for (int z = 0; z < DOZE; ++z) __builtin_amdgcn_s_sleep(127); }
  }
}
template <int N> __device__ __forceinline__ void sweep2(__amdgpu_buffer_rsrc_t rw, int off, int stride, u32 tag, u32x4 (&v)[N]) {
  for (u32 spins = 0; spins < SPIN_CAP; ++spins) {
    asm volatile("" ::: "memory");
    bool ok = true;
#pragma unroll
    for (int k = 0; k < N; ++k) v[k] = __builtin_amdgcn_raw_buffer_load_b128(rw, off + k * stride, 0, AUX_SC1);
#pragma unroll
    for (int k = 0; k < N; ++k) ok &= (v[k][1] == tag) & (v[k][3] == tag);
    if (__all(ok)) return;
  }
}

struct LnStats { float mb[2], den[2], rcp[2]; };
__device__ __forceinline__ LnStats ln_stats(const u32 (&xr)[2][5]) {
  LnStats s;
#pragma unroll
  for (int b = 0; b < 2; ++b) {
    float t = 0.f;
#pragma unroll
    for (int j = 0; j < 5; ++j) t += bflo(xr[b][j]) + bfhi(xr[b][j]);
    const float mean = wave_sum(t) * (1.f / DM);
    float v = 0.f;
#pragma unroll
    for (int j = 0; j < 5; ++j) { const float d0 = bflo(xr[b][j]) - mean, d1 = bfhi(xr[b][j]) - mean; v = fmaf(d0, d0, v); v = fmaf(d1, d1, v); }
    const float var = rbf(wave_sum(v) * (1.f / DM));
    s.mb[b] = rbf(mean);
    s.den[b] = rbf(__builtin_amdgcn_sqrtf(rbf(var + 1e-5f)));
    s.rcp[b] = __builtin_amdgcn_rcpf(s.den[b]);
  }
  return s;
}
__device__ __forceinline__ float fdiv(float a, float den, float rcp) { const float q0 = a * rcp; return fmaf(fmaf(-q0, den, a), rcp, q0); }
__device__ __forceinline__ float ln_apply(float x, float mb, float den, float rcp) { return rbf(fdiv(rbf(x - mb), den, rcp)); }

#define TS(i, dep) do { } while (0)

__global__ __launch_bounds__(NTHR, 2) void k_fused(const u16* __restrict__ x, const char* __restrict__ kc, const char* __restrict__ vc, const char* __restrict__ wq,
                                                   const char* __restrict__ wo, const char* __restrict__ wg, const char* __restrict__ wu, const char* __restrict__ wd,
                                                   u16* __restrict__ out, char* __restrict__ ws) {
  __shared__ __attribute__((aligned(16))) char lds[L_END];
  LAS char* L = (LAS char*)lds;
  const int blk = blockIdx.x, o = blk & 7, h = blk >> 3, b = o >> 2, sp = o & 3;
  const int tid = threadIdx.x, lane = tid & 63, wave = __builtin_amdgcn_readfirstlane(tid >> 6), r = lane >> 4, c = lane & 15;
  gu32* epoch = (gu32*)(ws + WS_EPOCH);
  gu64* cnt = (gu64*)(ws + WS_CNT + (b * NH + h) * 256);
  const __amdgpu_buffer_rsrc_t rw = __builtin_amdgcn_make_buffer_rsrc((void*)ws, 0, WS_END, 0x00020000);
  TS(0, lane);
  const u32 e0 = __hip_atomic_load(epoch, RLX_AGENT);
  const u32* x32 = (const u32*)x;
  u32 tag;

  if (__builtin_expect(wave < NW, 1)) {
    u32 xr[2][5];
#pragma unroll
    for (int bb = 0; bb < 2; ++bb)
#pragma unroll
      for (int j = 0; j < 5; ++j) xr[bb][j] = x32[bb * 320 + lane + 64 * j];
    const int cq = (lane >> 2) & 3, k0 = 80 * o + 4 * r + (lane & 3);
    u16 xo[2][5];
#pragma unroll
    for (int i = 0; i < 5; ++i) { xo[0][i] = x[k0 + 16 * i]; xo[1][i] = x[DM + k0 + 16 * i]; }
    SB();
    u32x4 wv[5];
    const char* wqp = wq + (size_t)k0 * (NH * DH * 2) + h * 256 + wave * 64 + cq * 16;
#pragma unroll
    for (int i = 0; i < 5; ++i) wv[i] = __builtin_nontemporal_load((const u32x4*)(wqp + (size_t)i * 16 * (NH * DH * 2)));
    SB();
    const __amdgpu_buffer_rsrc_t rk = __builtin_amdgcn_make_buffer_rsrc((void*)(kc + (size_t)(b * NH + h) * SLAB_B), 0, SLAB_B, 0x00020000);
    const __amdgpu_buffer_rsrc_t rv = __builtin_amdgcn_make_buffer_rsrc((void*)(vc + (size_t)(b * NH + h) * SLAB_B), 0, SLAB_B, 0x00020000);
    const int vo = lane * 16;
    const int tile0 = sp * NS * NW + wave;
#define LOADT(dst, rs, off) _Pragma("unroll") for (int i_ = 0; i_ < TL; ++i_) dst[i_] = __builtin_amdgcn_raw_buffer_load_b128(rs, vo + i_ * 1024, off, AUX_NT)
    u32x4 K[NPRE][TL], V[NPRE][TL];
    LOADT(K[0], rk, tile0 * TILE_B);
    LOADT(V[0], rv, tile0 * TILE_B);
    SB();
    TS(8, lane);
    const LnStats st = ln_stats(xr);
    TS(9, st.rcp[1]);
    float qa[16];
#pragma unroll
    for (int k = 0; k < 16; ++k) qa[k] = 0.f;
#pragma unroll
    for (int i = 0; i < 5; ++i) {
      const float x0 = ln_apply(bf2f(xo[0][i]), st.mb[0], st.den[0], st.rcp[0]), x1 = ln_apply(bf2f(xo[1][i]), st.mb[1], st.den[1], st.rcp[1]);
#pragma unroll
      for (int k = 0; k < 4; ++k) {
        const float w0 = bflo(wv[i][k]), w1 = bfhi(wv[i][k]);
        qa[2 * k] = fmaf(x0, w0, qa[2 * k]); qa[2 * k + 1] = fmaf(x0, w1, qa[2 * k + 1]);
        qa[8 + 2 * k] = fmaf(x1, w0, qa[8 + 2 * k]); qa[8 + 2 * k + 1] = fmaf(x1, w1, qa[8 + 2 * k + 1]);
      }
    }
    TS(10, qa[15]);
    {
#pragma unroll
      for (int k = 0; k < 8; ++k) qa[k] = fold32(qa[k], qa[8 + k]);
#pragma unroll
      for (int k = 0; k < 4; ++k) qa[k] = fold16(qa[k], qa[4 + k]);
#pragma unroll
      for (int k = 0; k < 2; ++k) { const float t1 = qa[k] + dpp<XOR2>(qa[k]), t2 = qa[2 + k] + dpp<XOR2>(qa[2 + k]); qa[k] = (lane & 2) ? t2 : t1; }
      const float t1 = qa[0] + dpp<XOR1>(qa[0]), t2 = qa[1] + dpp<XOR1>(qa[1]);
      const float s = (lane & 1) ? t2 : t1;
      tag = __builtin_amdgcn_readfirstlane(e0) + 1u;
      const int j = ((lane >> 4) & 1) * 4 + (lane & 3);
      put((gu64*)(ws + WS_QP) + (h * 8 + o) * 256 + (lane >> 5) * 128 + 32 * wave + 8 * cq + j, tag, s);
    }
    TS(11, lane);
#pragma unroll
    for (int p = 1; p < NPRE; ++p) {
      SB();
      if (p == 1) asm volatile("s_waitcnt vmcnt(5)" ::: "memory"); else asm volatile("s_waitcnt vmcnt(4)" ::: "memory");
      LOADT(K[p], rk, (tile0 + p * NW) * TILE_B);
      SB();
      if (p == 1) asm volatile("s_waitcnt vmcnt(5)" ::: "memory"); else asm volatile("s_waitcnt vmcnt(4)" ::: "memory");
      LOADT(V[p], rv, (tile0 + p * NW) * TILE_B);
    }
    SB();
    TS(12, lane);
    asm volatile("s_barrier" ::: "memory");
    const u32x4 qv = *(const LAS u32x4*)(L + L_QBF + c * 16);
    TS(2, qv[0]);
    float m = -INFINITY, l = 0.f;
    f32x2 ac[4];
#pragma unroll
    for (int k = 0; k < 4; ++k) ac[k] = f32x2{0.f, 0.f};
    constexpr float C2 = 0.088388347648318440f * 1.4426950408889634f;
#define TILE(KK, VV, REFILL, son) { \
      float xs[TL]; \
      _Pragma("unroll") for (int i = 0; i < TL; ++i) { \
        float d = dot2(KK[i][0], qv[0], 0.f); d = dot2(KK[i][1], qv[1], d); d = dot2(KK[i][2], qv[2], d); d = dot2(KK[i][3], qv[3], d); \
        xs[i] = d; asm volatile("" : "+v"(xs[i])); \
        if (REFILL) KK[i] = __builtin_amdgcn_raw_buffer_load_b128(rk, vo + i * 1024, son, AUX_NT); \
        SB(); } \
      _Pragma("unroll") for (int hh = 0; hh < 2; ++hh) { const float t1_ = xs[hh] + dpp<XOR8>(xs[hh]), t2_ = xs[hh + 2] + dpp<XOR8>(xs[hh + 2]); xs[hh] = (c & 8) ? t2_ : t1_; } \
      { const float t1_ = xs[0] + dpp<XOR7>(xs[0]), t2_ = xs[1] + dpp<XOR7>(xs[1]); xs[0] = (c & 4) ? t2_ : t1_; } \
      xs[0] += dpp<XOR2>(xs[0]); \
      const float sc = (xs[0] + dpp<XOR1>(xs[0])) * C2;                   \
      float pm = fmaxf(sc, dpp<ROR4>(sc)); pm = fmaxf(pm, dpp<XOR8>(pm)); \
      const float mn = fmaxf(m, pm); \
      const float alpha = __builtin_amdgcn_exp2f(m - mn), p = __builtin_amdgcn_exp2f(sc - mn); \
      l = fmaf(l, alpha, p); m = mn; \
      _Pragma("unroll") for (int k = 0; k < 4; ++k) ac[k] *= alpha; \
      PVSTEP(VV, 0, REFILL, son) PVSTEP(VV, 1, REFILL, son) PVSTEP(VV, 2, REFILL, son) PVSTEP(VV, 3, REFILL, son) }
#define PVSTEP(VV, i, REFILL, son) { const float pb = __builtin_bit_cast(float, __builtin_amdgcn_mov_dpp(__builtin_bit_cast(int, p), 0x150 + 4 * (i), 0xf, 0xf, true)); \
        _Pragma("unroll") for (int k = 0; k < 4; ++k) { ac[k] = __builtin_elementwise_fma(f32x2{pb, pb}, f32x2{bflo(VV[i][k]), bfhi(VV[i][k])}, ac[k]); asm volatile("" : "+v"(ac[k])); } \
        if (REFILL) VV[i] = __builtin_amdgcn_raw_buffer_load_b128(rv, vo + (i) * 1024, son, AUX_NT); \
        SB(); }
    { const int s4 = (tile0 + NPRE * NW) * TILE_B; TILE(K[0], V[0], true, s4) }
#pragma unroll
    for (int pf = 1; pf < NPRE; ++pf) TILE(K[pf], V[pf], false, 0)
    int t = NPRE, cur = tile0 + NPRE * NW, nxt = cur + NW;
    do {
      const bool dyn = t + 2 >= NS;
      u32x2 cl = {0u, 0u};
      if (dyn) { const u32 zero = 0; const u32x2 one = {1u, 0u}; u64 ex;
        asm volatile("s_mov_b64 %1, exec\n\ts_mov_b64 exec, 1\n\tglobal_atomic_add_x2 %0, %2, %3, %4 sc0\n\ts_mov_b64 exec, %1" : "=&v"(cl), "=&s"(ex) : "v"(zero), "v"(one), "s"(cnt) : "memory"); }
      SB();
      const int son = nxt < NTILES ? nxt * TILE_B : 0x7ff00000;
      TILE(K[0], V[0], true, son)
      cur = nxt;
      if (dyn) {
        asm volatile("s_waitcnt vmcnt(%1)" : "+v"(cl) : "n"(2 * TL));
        u32 got = __builtin_amdgcn_readfirstlane(cl[0]);
        if (__builtin_expect(__builtin_amdgcn_readfirstlane(cl[1]) != tag, 0)) {
          got = NTILES;
          for (u32 spins = 0; spins < SPIN_CAP; ++spins) {
            u64 old = 0;
            if (lane == 0) old = __hip_atomic_fetch_add(cnt, 1ull, RLX_AGENT);
            if ((u32)__builtin_amdgcn_readfirstlane((u32)(old >> 32)) == tag) { got = __builtin_amdgcn_readfirstlane((u32)old); break; }
          }
        }
        nxt = POOL0 + (int)min(got, (u32)NTILES);
      } else nxt = nxt + NW;
      ++t;
    } while (cur < NTILES);
    TS(3, m);
    __builtin_amdgcn_s_waitcnt(0x0F70);
    {
      const float M = x32_max(x16_max(m));
      const float sc = __builtin_amdgcn_exp2f(m - M);
      l = wave_sum(l * sc) * 0.25f;
      float a[8];
#pragma unroll
      for (int k = 0; k < 8; ++k) a[k] = ac[k >> 1][k & 1] * sc;
#pragma unroll
      for (int k = 0; k < 4; ++k) a[k] = fold32(a[k], a[4 + k]);
#pragma unroll
      for (int k = 0; k < 2; ++k) a[k] = fold16(a[k], a[2 + k]);
      LAS float* rec = (LAS float*)(L + L_MRG + wave * 528);
      *(LAS f32x2*)(rec + 4 + 8 * c + 4 * (lane >> 5) + 2 * (r & 1)) = f32x2{a[0], a[1]};
      if (lane == 0) *(LAS f32x2*)rec = f32x2{M, l};
    }
    LDS_BARRIER();
    if (tid < 128) {
      const LAS float* rec = (const LAS float*)(L + L_MRG);
      float M = rec[0];
#pragma unroll
      for (int w = 1; w < NW; ++w) M = fmaxf(M, rec[w * 132]);
      float Ls = 0.f, ov = 0.f;
#pragma unroll
      for (int w = 0; w < NW; ++w) { const float wt = __builtin_amdgcn_exp2f(rec[w * 132] - M); Ls = fmaf(wt, rec[w * 132 + 1], Ls); ov = fmaf(wt, rec[w * 132 + 4 + tid], ov); }
      __builtin_amdgcn_raw_buffer_store_b128(u32x4{fbits(ov), tag, fbits((tid & 1) ? Ls : M), tag}, rw, WS_OP + ((h * 8 + o) * 128 + tid) * 16, 0, AUX_SC1);
    }
    TS(4, lane);
  } else {
    tag = __builtin_amdgcn_readfirstlane(e0) + 1u;
    if (sp == 0 && lane == 0) __hip_atomic_store(cnt, (u64)tag << 32, RLX_AGENT);
    {
      u32x4 v[8];
      const int base = WS_QP + ((h * 8 + r) * 256 + b * 128 + 8 * c) * 8;
      for (u32 spins = 0; spins < SPIN_CAP; ++spins) {
        asm volatile("" ::: "memory");
        bool ok = true;
#pragma unroll
        for (int k = 0; k < 8; ++k) v[k] = __builtin_amdgcn_raw_buffer_load_b128(rw, base + (k & 3) * 16, (k >> 2) * 8192, AUX_SC1);
#pragma unroll
        for (int k = 0; k < 8; ++k) ok &= (v[k][1] == tag) & (v[k][3] == tag);
        if (__all(ok)) break;
      }
      float a[8];
#pragma unroll
      for (int k = 0; k < 4; ++k) { a[2 * k] = bitsf(v[k][0]) + bitsf(v[4 + k][0]); a[2 * k + 1] = bitsf(v[k][2]) + bitsf(v[4 + k][2]); }
#pragma unroll
      for (int k = 0; k < 4; ++k) a[k] = fold32(a[k], a[4 + k]);
#pragma unroll
      for (int k = 0; k < 2; ++k) a[k] = fold16(a[k], a[2 + k]);
      *(LAS u32*)(L + L_QBF + (8 * c + 4 * (lane >> 5) + 2 * (r & 1)) * 2) = pack2(a[0], a[1]);
    }
    TS(8, lane);
    LDS_BARRIER();
    const bool ffn = h < NCB;
    u32 xr[2][5], xs[2] = {0u, 0u};
    u32x4 gv[20];
    if (ffn) {
#pragma unroll
      for (int bb = 0; bb < 2; ++bb) {
#pragma unroll
        for (int j = 0; j < 5; ++j) xr[bb][j] = x32[bb * 320 + lane + 64 * j];
        if (lane < 40) xs[bb] = x32[bb * 320 + 40 * o + lane];
      }
      const char* gp = (c < 8 ? wg : wu) + (size_t)(80 * o + r) * (DF * 2) + h * 128 + (c & 7) * 16;
#pragma unroll
      for (int i = 0; i < 20; ++i) gv[i] = __builtin_nontemporal_load((const u32x4*)(gp + (size_t)i * 4 * (DF * 2)));
      const LnStats st = ln_stats(xr);
      if (lane < 40) {
        LAS f32x2* xn = (LAS f32x2*)(L + L_XN);
        xn[2 * lane] = f32x2{ln_apply(bflo(xs[0]), st.mb[0], st.den[0], st.rcp[0]), ln_apply(bflo(xs[1]), st.mb[1], st.den[1], st.rcp[1])};
        xn[2 * lane + 1] = f32x2{ln_apply(bfhi(xs[0]), st.mb[0], st.den[0], st.rcp[0]), ln_apply(bfhi(xs[1]), st.mb[1], st.den[1], st.rcp[1])};
      }
      asm volatile("s_waitcnt lgkmcnt(0)" ::: "memory");
      float ga[2][8];
#pragma unroll
      for (int k = 0; k < 8; ++k) { ga[0][k] = 0.f; ga[1][k] = 0.f; }
#pragma unroll
      for (int i = 0; i < 20; ++i) {
        const f32x2 xv = ((const LAS f32x2*)(L + L_XN))[4 * i + r];
#pragma unroll
        for (int k = 0; k < 4; ++k) {
          const float w0 = bflo(gv[i][k]), w1 = bfhi(gv[i][k]);
          ga[0][2 * k] = fmaf(xv[0], w0, ga[0][2 * k]); ga[0][2 * k + 1] = fmaf(xv[0], w1, ga[0][2 * k + 1]);
          ga[1][2 * k] = fmaf(xv[1], w0, ga[1][2 * k]); ga[1][2 * k + 1] = fmaf(xv[1], w1, ga[1][2 * k + 1]);
        }
      }
#pragma unroll
      for (int k = 0; k < 8; ++k) { ga[0][k] = x32_sum(x16_sum(ga[0][k])); ga[1][k] = x32_sum(x16_sum(ga[1][k])); }
      gu64* gpub = (gu64*)(ws + WS_GP) + (h * 8 + o) * 256 + (r >> 1) * 128 + (8 * (c & 7) + 4 * (r & 1)) * 2 + (c >> 3);
#pragma unroll
      for (int k = 0; k < 4; ++k) { const float v = r == 0 ? ga[0][k] : r == 1 ? ga[0][4 + k] : r == 2 ? ga[1][k] : ga[1][4 + k]; put(gpub + 2 * k, tag, v); }
    }
#pragma unroll
    for (int j = 0; j < 20; ++j) { const int q = 64 * j + lane, row = q / 10, cg = q - row * 10;
      __builtin_amdgcn_global_load_lds((const GAS u32*)(wo + (size_t)(h * 128 + row) * (DM * 2) + o * 160 + cg * 16), (LAS u32*)(L + L_WO + j * 1024), 16, 0, AUX_NT); }
    if (ffn) {
#pragma unroll
      for (int j = 0; j < 10; ++j) { const int q = 64 * j + lane, row = q / 10, cg = q - row * 10;
        __builtin_amdgcn_global_load_lds((const GAS u32*)(wd + (size_t)(h * 64 + row) * (DM * 2) + o * 160 + cg * 16), (LAS u32*)(L + L_WD + j * 1024), 16, 0, AUX_NT);
      }
      for (int z = 0; z < PRESLEEP; ++z) __builtin_amdgcn_s_sleep(127);
      float gs[4] = {0.f, 0.f, 0.f, 0.f};
      for (int p = 0; p < 8; ++p) {
        float v[4];
        sweep<4>((gu64*)(ws + WS_GP) + (h * 8 + p) * 256 + 4 * lane, 1, tag, v, true, true);
#pragma unroll
        for (int k = 0; k < 4; ++k) gs[k] += v[k];
      }
      float hv[2];
#pragma unroll
      for (int k = 0; k < 2; ++k) {
        const float g = rbf(gs[2 * k]), u = rbf(gs[2 * k + 1]);
        const float z = 0.7978845608028654f * (g + 0.044715f * g * g * g);
        const float th = 1.f - 2.f / (__expf(2.f * z) + 1.f);
        hv[k] = rbf(rbf(0.5f * g * (1.f + th)) * u);
      }
      {
        LAS float* hs = (LAS float*)(L + L_HS);
        const int bb = lane >> 5, col = (2 * lane) & 63;
        hs[col * 2 + bb] = hv[0]; hs[(col + 1) * 2 + bb] = hv[1];
      }
      __builtin_amdgcn_s_waitcnt(0x0070);
      if (lane < 40) {
        float y00 = 0.f, y01 = 0.f, y10 = 0.f, y11 = 0.f;
#pragma unroll 8
        for (int f = 0; f < 64; ++f) {
          const u32 w = *(const LAS u32*)(L + L_WD + f * 160 + lane * 4);
          const f32x2 hh = ((const LAS f32x2*)(L + L_HS))[f];
          y00 = fmaf(hh[0], bflo(w), y00); y01 = fmaf(hh[0], bfhi(w), y01); y10 = fmaf(hh[1], bflo(w), y10); y11 = fmaf(hh[1], bfhi(w), y11);
        }
        const int yo = WS_YP + ((o * NCB + h) * 160 + 4 * lane) * 8;
        __builtin_amdgcn_raw_buffer_store_b128(u32x4{fbits(y00), tag, fbits(y10), tag}, rw, yo, 0, AUX_SC1);
        __builtin_amdgcn_raw_buffer_store_b128(u32x4{fbits(y01), tag, fbits(y11), tag}, rw, yo + 16, 0, AUX_SC1);
      }
    }
    {
      float gsum[3];
      for (int z = 0; z < PRESLEEP; ++z) __builtin_amdgcn_s_sleep(127);
#pragma unroll
      for (int ps = 0; ps < 3; ++ps) {
        const int k = 2 * ps + (lane >> 5), cb = lane & 31;
        const bool act = k < 5 && cb < NCB;
        float v[1] = {0.f};
        sweep<1>((gu64*)(ws + WS_YP) + (o * NCB + (act ? cb : 0)) * 160 + 5 * h + (act ? k : 0), 1, tag, v, act, true);
        gsum[ps] = x16_sum(row_sum(act ? v[0] : 0.f));
      }
      if ((lane & 31) == 0) {
#pragma unroll
        for (int ps = 0; ps < 3; ++ps) { const int k = 2 * ps + (lane >> 5);
          if (k < 5) { const float y = rbf(gsum[ps]); ((LAS float*)(L + L_GATE))[k] = rbf(y / (1.f + __expf(-y))); } }
      }
    }
    __builtin_amdgcn_s_waitcnt(0x0F70);
    TS(9, lane);
    LDS_BARRIER();
  }
  s16x8 bw[4];
  {
    const int g = lane >> 4, q = (lane & 15) >> 2, p = lane & 3;
#pragma unroll
    for (int s = 0; s < 4; ++s) {
      const LAS char* a0 = L + L_WO + (32 * s + 8 * g + q) * 160 + (16 * wave + 4 * p) * 2;
      const s16x4 lo = __builtin_bit_cast(s16x4, __builtin_amdgcn_ds_read_tr16_b64_v4i16((LAS s16x4*)a0));
      const s16x4 hi = __builtin_bit_cast(s16x4, __builtin_amdgcn_ds_read_tr16_b64_v4i16((LAS s16x4*)(a0 + 4 * 160)));
      bw[s] = s16x8{lo[0], lo[1], lo[2], lo[3], hi[0], hi[1], hi[2], hi[3]};
    }
  }
  const int kf = 2 * wave + (lane >> 5);
  const bool actf = wave < 3 && kf < 5;
  const int jf = 5 * h + (actf ? kf : 0), colf = 80 * o + (jf >> 1), bf = jf & 1;
  const float xv = bf2f(x[bf * DM + colf]);
  const float gate = ((const LAS float*)(L + L_GATE))[actf ? kf : 0];
  if (wave < 4) {
    const int bb = tid >> 7, d = tid & 127;
    u32x4 v[4];
    sweep2<4>(rw, WS_OP + ((h * 8 + bb * 4) * 128 + d) * 16, 2048, tag, v);
    float Mv[4], Lv[4];
#pragma unroll
    for (int s = 0; s < 4; ++s) { const float ml = bitsf(v[s][2]), ot = dpp<XOR1>(ml); Mv[s] = (lane & 1) ? ot : ml; Lv[s] = (lane & 1) ? ml : ot; }
    const float M = fmaxf(fmaxf(Mv[0], Mv[1]), fmaxf(Mv[2], Mv[3]));
    float Ls = 0.f, a = 0.f;
#pragma unroll
    for (int s = 0; s < 4; ++s) { const float wt = __builtin_amdgcn_exp2f(Mv[s] - M); Ls = fmaf(wt, Lv[s], Ls); a = fmaf(wt, bitsf(v[s][0]), a); }
    *(LAS u16*)(L + L_OF + bb * 256 + d * 2) = f2bf(fdiv(a, Ls, __builtin_amdgcn_rcpf(Ls)));
  }
  TS(5, lane);
  LDS_BARRIER();
  {
    f32x4 acc = {0.f, 0.f, 0.f, 0.f};
#pragma unroll
    for (int s = 0; s < 4; ++s) {
      const u32x4 a = *(const LAS u32x4*)(L + L_OF + (lane & 1) * 256 + (32 * s + 8 * (lane >> 4)) * 2);
      acc = __builtin_amdgcn_mfma_f32_16x16x32_bf16(__builtin_bit_cast(bf16x8, a), __builtin_bit_cast(bf16x8, bw[s]), acc, 0, 0, 0);
    }
    if (lane < 16) __builtin_amdgcn_raw_buffer_store_b128(u32x4{fbits(acc[0]), tag, fbits(acc[1]), tag}, rw, WS_SP + ((o * NH + h) * 160 + (16 * wave + lane) * 2) * 8, 0, AUX_SC1);
  }
  TS(6, lane);
  if (wave < 3) {
    float v[1] = {0.f};
    sweep<1>((gu64*)(ws + WS_SP) + (o * NH + (lane & 31)) * 160 + jf, 1, tag, v, actf, false);
    const float s = rbf(x16_sum(row_sum(v[0])));
    if (actf && (lane & 31) == 0) out[bf * DM + colf] = f2bf(xv + rbf(s * gate));
    if (blk == 0 && tid == 0) __hip_atomic_store(epoch, tag, RLX_AGENT);
  }
  TS(7, lane);
}

extern "C" void kernel_launch(void* const* d_in, const int* in_sizes, int n_in, void* d_out, int out_size, void* d_ws, size_t ws_size, hipStream_t stream) {
  k_fused<<<256, NTHR, 0, stream>>>((const u16*)d_in[0], (const char*)d_in[1], (const char*)d_in[2], (const char*)d_in[3], (const char*)d_in[4],
                                    (const char*)d_in[6], (const char*)d_in[7], (const char*)d_in[8], (u16*)d_out, (char*)d_ws);
}
```

```cpp
#include <hip/hip_runtime.h>
#include <hip/hip_bf16.h>
#include <stdint.h>

typedef unsigned int u32;
typedef unsigned long long u64;
typedef unsigned short u16;
typedef u32 u32x4 __attribute__((ext_vector_type(4)));
typedef u32 u32x2 __attribute__((ext_vector_type(2)));
typedef float f32x4 __attribute__((ext_vector_type(4)));
typedef float f32x2 __attribute__((ext_vector_type(2)));
typedef short s16x4 __attribute__((ext_vector_type(4)));
typedef short s16x8 __attribute__((ext_vector_type(8)));
typedef __bf16 bf16x2 __attribute__((ext_vector_type(2)));
typedef __bf16 bf16x8 __attribute__((ext_vector_type(8)));
#define GAS __attribute__((address_space(1)))
#define LAS __attribute__((address_space(3)))
typedef GAS u32 gu32;
typedef GAS u64 gu64;
#define RLX_AGENT __ATOMIC_RELAXED, __HIP_MEMORY_SCOPE_AGENT

constexpr int DM = 640, NH = 32, SK = 16384, DH = 128, DF = 1728;
constexpr int NW = 4;
constexpr int NTHR = (NW + 1) * 64;
constexpr int TL = 4;
constexpr int TILE_B = TL * 1024;
constexpr int SLAB_B = SK * DH * 2;
constexpr int NTILES = SLAB_B / TILE_B;
constexpr int NS = 56;
constexpr int POOL0 = 4 * NW * NS;
constexpr int NCH = (NTILES - POOL0) / NW;
constexpr int NPK = 8;
constexpr int LEAD = 32;
constexpr u32 PHASE_T0 = 1710, PHASE_T = 1540;
constexpr int PACE = 55;
constexpr int AHEAD = 7;
constexpr int DEMAND = 2;
constexpr int LATE = 8;
constexpr int NCB = DF / 64;
constexpr u32 Q_EMPTY = 0xffffffffu;
constexpr u32 SPIN_CAP = 1u << 16;
constexpr int AUX_NT = 2, AUX_SC1 = 16;
static_assert(POOL0 <= NTILES && NS >= NPK + 2 && LEAD >= NPK + 2 && (LEAD & (LEAD - 1)) == 0, "tile plan");

constexpr int WS_EPOCH = 0;
constexpr int WS_CNT = 4096;
constexpr int WS_QP = 32768;
constexpr int WS_GP = WS_QP + NH * 8 * 256 * 8;
constexpr int WS_YP = WS_GP + NCB * 2 * 256 * 8;
constexpr int WS_OP = WS_YP + NCB * 1280 * 8;
constexpr int WS_SP = WS_OP + NH * 8 * 256 * 8;
constexpr int WS_END = WS_SP + NH * 1280 * 8;

constexpr int L_W = 0;
constexpr int L_QRED = L_W + 49152;
constexpr int L_QBF = L_QRED + 4096;
constexpr int L_XN = L_QBF + 256;
constexpr int L_GT = L_XN + 2560;
constexpr int L_HS = L_GT + 1024;
constexpr int L_MRG = L_HS + 512;
constexpr int L_OF = L_MRG + NW * 528;
constexpr int L_NEED = L_OF + 512;
constexpr int L_PRE = L_NEED + 16;
constexpr int L_NPRE = L_PRE + 16;
constexpr int L_Q = L_NPRE + 16;
constexpr int L_SC = L_Q + ((NCH + 8) * 4 + 15) / 16 * 16;
constexpr int L_END = L_SC + NW * LEAD * 256;
static_assert(L_END > 80 * 1024 && L_END <= 160 * 1024, "more than half of a CU's LDS: one workgroup per CU, all 256 resident");

__device__ __forceinline__ float bf2f(u16 v) { return __builtin_bit_cast(float, (u32)v << 16); }
__device__ __forceinline__ float bflo(u32 v) { return __builtin_bit_cast(float, v << 16); }
__device__ __forceinline__ float bfhi(u32 v) { return __builtin_bit_cast(float, v & 0xffff0000u); }
__device__ __forceinline__ u16 f2bf(float f) { __bf16 b = (__bf16)f; return __builtin_bit_cast(u16, b); }
__device__ __forceinline__ u32 pack2(float lo, float hi) { return (u32)f2bf(lo) | ((u32)f2bf(hi) << 16); }
__device__ __forceinline__ float rbf(float f) { return bf2f(f2bf(f)); }
__device__ __forceinline__ u32 fbits(float f) { return __builtin_bit_cast(u32, f); }
__device__ __forceinline__ float bitsf(u32 u) { return __builtin_bit_cast(float, u); }
__device__ __forceinline__ float dot2(u32 k, u32 q, float acc) {
  return __builtin_amdgcn_fdot2_f32_bf16(__builtin_bit_cast(bf16x2, k), __builtin_bit_cast(bf16x2, q), acc, false);
}
template <int CTRL> __device__ __forceinline__ float dpp(float x) {
  return __builtin_bit_cast(float, __builtin_amdgcn_mov_dpp(__builtin_bit_cast(int, x), CTRL, 0xf, 0xf, true));
}
constexpr int XOR1 = 0xB1, XOR2 = 0x4E, XOR7 = 0x141, XOR8 = 0x128, ROR4 = 0x124, ROR2 = 0x122, ROR1 = 0x121, BCAST = 0x150;
__device__ __forceinline__ float fold16(float a, float b) { auto s = __builtin_amdgcn_permlane16_swap(fbits(a), fbits(b), false, false); return bitsf(s[0]) + bitsf(s[1]); }
__device__ __forceinline__ float fold32(float a, float b) { auto s = __builtin_amdgcn_permlane32_swap(fbits(a), fbits(b), false, false); return bitsf(s[0]) + bitsf(s[1]); }
__device__ __forceinline__ float x16_max(float x) { auto s = __builtin_amdgcn_permlane16_swap(fbits(x), fbits(x), false, false); return fmaxf(bitsf(s[0]), bitsf(s[1])); }
__device__ __forceinline__ float x32_max(float x) { auto s = __builtin_amdgcn_permlane32_swap(fbits(x), fbits(x), false, false); return fmaxf(bitsf(s[0]), bitsf(s[1])); }
__device__ __forceinline__ float row_sum(float x) { x += dpp<XOR8>(x); x += dpp<ROR4>(x); x += dpp<ROR2>(x); x += dpp<ROR1>(x); return x; }
__device__ __forceinline__ float half_sum(float x) { x = row_sum(x); return fold16(x, x); }
__device__ __forceinline__ float wave_sum(float x) { x = half_sum(x); return fold32(x, x); }
#define LDS_BARRIER() asm volatile("s_waitcnt lgkmcnt(0)\n\ts_barrier" ::: "memory")
#define SB() __builtin_amdgcn_sched_barrier(0)
#define WAIT_VM0() __builtin_amdgcn_s_waitcnt(0x0F70)

template <int N> __device__ __forceinline__ void sweep2(__amdgpu_buffer_rsrc_t rw, int off, int stride, u32 tag, u32x4 (&v)[N], int nap) {
#pragma unroll
  for (int k = 0; k < N; ++k) v[k] = __builtin_amdgcn_raw_buffer_load_b128(rw, off + k * stride, 0, AUX_SC1);
  for (u32 spins = 0; spins < SPIN_CAP; ++spins) {
    bool ok = true;
#pragma unroll
    for (int k = 0; k < N; ++k) ok &= (v[k][1] == tag) & (v[k][3] == tag);
    if (__all(ok)) return;
    if (nap) __builtin_amdgcn_s_sleep(40);
    asm volatile("" : "+v"(off) :: "memory");
#pragma unroll
    for (int k = 0; k < N; ++k) v[k] = __builtin_amdgcn_raw_buffer_load_b128(rw, off + k * stride, 0, AUX_SC1);
  }
}

struct LnStats { float mb[2], den[2], rcp[2]; };
__device__ __forceinline__ LnStats ln_stats(const u32 (&xr)[2][5]) {
  LnStats s;
#pragma unroll
  for (int b = 0; b < 2; ++b) {
    float t = 0.f;
#pragma unroll
    for (int j = 0; j < 5; ++j) t += bflo(xr[b][j]) + bfhi(xr[b][j]);
    const float mean = wave_sum(t) * (1.f / DM);
    float v = 0.f;
#pragma unroll
    for (int j = 0; j < 5; ++j) { const float d0 = bflo(xr[b][j]) - mean, d1 = bfhi(xr[b][j]) - mean; v = fmaf(d0, d0, v); v = fmaf(d1, d1, v); }
    const float var = rbf(wave_sum(v) * (1.f / DM));
    s.mb[b] = rbf(mean);
    s.den[b] = rbf(__builtin_amdgcn_sqrtf(rbf(var + 1e-5f)));
    s.rcp[b] = __builtin_amdgcn_rcpf(s.den[b]);
  }
  return s;
}
__device__ __forceinline__ float fdiv(float a, float den, float rcp) { const float q0 = a * rcp; return fmaf(fmaf(-q0, den, a), rcp, q0); }
__device__ __forceinline__ float ln_apply(float x, float mb, float den, float rcp) { return rbf(fdiv(rbf(x - mb), den, rcp)); }

__global__ __launch_bounds__(NTHR, 2) void k_fused(const u16* __restrict__ x, const char* __restrict__ kc, const char* __restrict__ vc, const char* __restrict__ wq,
                                                   const char* __restrict__ wo, const char* __restrict__ wg, const char* __restrict__ wu, const char* __restrict__ wd,
                                                   u16* __restrict__ out, char* ws) {
  __shared__ __attribute__((aligned(16))) char lds[L_END];
  LAS char* L = (LAS char*)lds;
  const int blk = blockIdx.x, xo = blk & 7, h = blk >> 3;
  const int b = (xo ^ (xo >> 2)) & 1, sp = xo >> 1, o = 4 * b + sp;
  const int tid = threadIdx.x, lane = tid & 63, wave = __builtin_amdgcn_readfirstlane(tid >> 6), r = lane >> 4, c = lane & 15;
  int woj, fh, wd0, wdn;
#define ROLES() { int o_ = o, h_ = h; asm volatile("" : "+s"(o_), "+s"(h_));     \
    const int odd = h_ & 1, sp_ = o_ & 3; \
    woj = o_ < 4 ? (sp_ < 3 - odd ? sp_ : -1) : (sp_ < 2 + odd ? 3 - odd + sp_ : -1); \
    fh = h_ >= NCB ? -1 : o_ == 3 - odd ? 0 : o_ == 6 + odd ? 1 : -1; \
    wd0 = fh == 0 ? 0 : 2 + odd; wdn = fh == 0 ? 2 + odd : 3 - odd; }
  gu32* epoch = (gu32*)(ws + WS_EPOCH);
  gu64* cnt = (gu64*)(ws + WS_CNT + (b * NH + h) * 256);
  const __amdgpu_buffer_rsrc_t rw = __builtin_amdgcn_make_buffer_rsrc((void*)ws, 0, WS_END, 0x00020000);
  const u64 t_start = __builtin_amdgcn_s_memrealtime();
  const u32 e0 = __hip_atomic_load(epoch, RLX_AGENT);
  const u32* x32 = (const u32*)x;
  u32 tag;
  const int kf = 2 * wave + (lane >> 5);
  const bool actf = wave < 3 && kf < 5;
  const int jf = 5 * blk + (actf ? kf : 0), colf = jf >> 1, bf = jf & 1;
  u16 xres;

  if (__builtin_expect(wave < NW, 1)) {
    u32 xr[2][5];
#pragma unroll
    for (int bb = 0; bb < 2; ++bb)
#pragma unroll
      for (int j = 0; j < 5; ++j) xr[bb][j] = x32[bb * 320 + lane + 64 * j];
    const int k0 = 80 * o + 20 * wave + r;
    const char* wqp = wq + (size_t)k0 * (NH * DH * 2) + h * 256 + c * 16;
    u16 xq[2][5];
#pragma unroll
    for (int i = 0; i < 5; ++i) { xq[0][i] = x[k0 + 4 * i]; xq[1][i] = x[DM + k0 + 4 * i]; }
    SB();
    u32x4 wv[5];
#pragma unroll
    for (int i = 0; i < 5; ++i) wv[i] = __builtin_nontemporal_load((const u32x4*)(wqp + (size_t)i * 4 * (NH * DH * 2)));
    SB();
    const char* kb = kc + (size_t)(b * NH + h) * SLAB_B;
    const char* vb = vc + (size_t)(b * NH + h) * SLAB_B;
    const __amdgpu_buffer_rsrc_t rk = __builtin_amdgcn_make_buffer_rsrc((void*)kb, 0, SLAB_B, 0x00020000);
    const int vo = lane * 16;
    const int tile0 = sp * NS * NW + wave;
#define LOADT(dst, rs, off) _Pragma("unroll") for (int i_ = 0; i_ < TL; ++i_) dst[i_] = __builtin_amdgcn_raw_buffer_load_b128(rs, vo + i_ * 1024, off, AUX_NT)
    u32x4 A[TL], B[TL], E[NPK - 2][TL];
    LOADT(A, rk, tile0 * TILE_B);
    LOADT(B, rk, (tile0 + NW) * TILE_B);
    xres = x[bf * DM + colf];
    SB();
    const LnStats st = ln_stats(xr);
    float qa[16];
#pragma unroll
    for (int k = 0; k < 16; ++k) qa[k] = 0.f;
#pragma unroll
    for (int i = 0; i < 5; ++i) {
      const float x0 = ln_apply(bf2f(xq[0][i]), st.mb[0], st.den[0], st.rcp[0]), x1 = ln_apply(bf2f(xq[1][i]), st.mb[1], st.den[1], st.rcp[1]);
#pragma unroll
      for (int k = 0; k < 4; ++k) {
        const float w0 = bflo(wv[i][k]), w1 = bfhi(wv[i][k]);
        qa[2 * k] = fmaf(x0, w0, qa[2 * k]); qa[2 * k + 1] = fmaf(x0, w1, qa[2 * k + 1]);
        qa[8 + 2 * k] = fmaf(x1, w0, qa[8 + 2 * k]); qa[8 + 2 * k + 1] = fmaf(x1, w1, qa[8 + 2 * k + 1]);
      }
    }
    {
#pragma unroll
      for (int k = 0; k < 8; ++k) qa[k] = fold32(qa[k], qa[8 + k]);
#pragma unroll
      for (int k = 0; k < 4; ++k) qa[k] = fold16(qa[k], qa[4 + k]);
      *(LAS f32x4*)(L + L_QRED + wave * 1024 + (lane >> 5) * 512 + (8 * c + 4 * (r & 1)) * 4) = f32x4{qa[0], qa[1], qa[2], qa[3]};
      tag = __builtin_amdgcn_readfirstlane(e0) + 1u;
      asm volatile("" :: "s"(tag));
      LDS_BARRIER();
      const LAS float* qr = (const LAS float*)(L + L_QRED);
      const float s = (qr[tid] + qr[256 + tid]) + (qr[512 + tid] + qr[768 + tid]);
      __hip_atomic_store((gu64*)(ws + WS_QP) + (h * 8 + o) * 256 + tid, ((u64)tag << 32) | fbits(s), RLX_AGENT);
    }
#pragma unroll
    for (int p = 0; p < NPK - 2; ++p) {
      SB();
      if (p == 0) asm volatile("s_waitcnt vmcnt(6)" ::: "memory"); else if (p == 1) asm volatile("s_waitcnt vmcnt(5)" ::: "memory"); else asm volatile("s_waitcnt vmcnt(4)" ::: "memory");
      LOADT(E[p], rk, (tile0 + (p + 2) * NW) * TILE_B);
    }
    SB();
    asm volatile("s_barrier" ::: "memory");
    const u32x4 qv = *(const LAS u32x4*)(L + L_QBF + c * 16);
    float mrun = -INFINITY, m = -INFINITY, l = 0.f;
    f32x2 ac[4];
#pragma unroll
    for (int k = 0; k < 4; ++k) ac[k] = f32x2{0.f, 0.f};
    constexpr float SCALE = 0.088388347648318440f, LOG2E = 1.4426950408889634f;
    LAS float* scq = (LAS float*)(L + L_SC + wave * (LEAD * 256)) + lane;
    int ck = 0, cv = 0;
#define QKTILE(R) { \
      float xs[TL]; \
      _Pragma("unroll") for (int i = 0; i < TL; ++i) { \
        float d = dot2(R[i][0], qv[0], 0.f); d = dot2(R[i][1], qv[1], d); d = dot2(R[i][2], qv[2], d); d = dot2(R[i][3], qv[3], d); \
        xs[i] = d; } \
      _Pragma("unroll") for (int hh = 0; hh < 2; ++hh) { const float t1_ = xs[hh] + dpp<XOR8>(xs[hh]), t2_ = xs[hh + 2] + dpp<XOR8>(xs[hh + 2]); xs[hh] = (c & 8) ? t2_ : t1_; } \
      { const float t1_ = xs[0] + dpp<XOR7>(xs[0]), t2_ = xs[1] + dpp<XOR7>(xs[1]); xs[0] = (c & 4) ? t2_ : t1_; } \
      xs[0] += dpp<XOR2>(xs[0]); \
      const float sc = rbf(rbf(xs[0] + dpp<XOR1>(xs[0])) * SCALE) * LOG2E; \
      scq[(ck & (LEAD - 1)) * 64] = sc; ++ck; \
      float pm = fmaxf(sc, dpp<ROR4>(sc)); pm = fmaxf(pm, dpp<XOR8>(pm)); \
      mrun = fmaxf(mrun, pm); }
#define PVSTEP(R, i) { const float pb = dpp<BCAST + 4 * (i)>(p); \
        _Pragma("unroll") for (int k = 0; k < 4; ++k) ac[k] = __builtin_elementwise_fma(f32x2{pb, pb}, f32x2{bflo(R[i][k]), bfhi(R[i][k])}, ac[k]); }
#define PVTILE(R, sc) { \
      ++cv; \
      const float ms = mrun == -INFINITY ? 0.f : mrun; \
      const float alpha = __builtin_amdgcn_exp2f(m - ms), p = __builtin_amdgcn_exp2f(sc - ms); \
      l = fmaf(l, alpha, p); m = mrun; \
      _Pragma("unroll") for (int k = 0; k < 4; ++k) ac[k] *= alpha; \
      PVSTEP(R, 0) PVSTEP(R, 1) PVSTEP(R, 2) PVSTEP(R, 3) }
    int nk = NPK, nv = 0;
    int idk = tile0 + NPK * NW, idp = 0;
    int npre = 0, done = 0, tA = 0, tB = 0;
    u32 phase = 0, tsw = (u32)t_start + PHASE_T0, now = (u32)t_start, tnext = (u32)t_start;
    u32 vq = Q_EMPTY, vn = 0, vv = 0;
#define STEP(R, tR) { \
      const float scn = scq[(cv & (LEAD - 1)) * 64];     \
      if (nk >= NS - 14) {     \
        if (idk < 0) { const u32 g = __builtin_amdgcn_readfirstlane(vq); if (g != Q_EMPTY) idk = g < (u32)NTILES ? (int)g + wave : NTILES; } \
        npre = __builtin_amdgcn_readfirstlane(vn); idp = __builtin_amdgcn_readfirstlane(vv) + wave; } \
      { const u32 flip = ((now - tsw) >> 31) ^ 1u; phase ^= flip; tsw += flip * PHASE_T; } \
        \
      const u32 vok = (u32)(nv - nk) >> 31, phased = (u32)(nk - NS - npre) >> 31; \
      const u32 room = phased ? (u32)(nk - nv - (LEAD - 2)) >> 31 : vok ^ 1u; \
      const u32 kok = room & ((u32)(idk - NTILES) >> 31) & (((u32)idk >> 31) ^ 1u); \
      const u32 wantk = phased ? phase ^ 1u : vok ^ 1u; \
      const u32 isk = kok & (wantk | (vok ^ 1u)), isv = vok & (isk ^ 1u); \
      const int idn = isk ? idk : nv < NS ? tile0 + nv * NW : idp; \
      int so = (isk | isv) ? idn * TILE_B : 0x7ff00000;     \
      const char* bp = isk ? kb : vb; \
      asm volatile("" : "+s"(so), "+s"(bp));                \
      const __amdgpu_buffer_rsrc_t rr = __builtin_amdgcn_make_buffer_rsrc((void*)bp, 0, SLAB_B, 0x00020000); \
      SB(); \
      if (tR == 1) { PVTILE(R, scn) asm volatile("" : "+v"(ac[0]), "+v"(ac[1]), "+v"(ac[2]), "+v"(ac[3])); } \
      SB(); \
      if (tR == 0) { QKTILE(R) asm volatile("" :: "v"(mrun)); } \
      SB(); \
      do { now = (u32)__builtin_amdgcn_s_memrealtime(); } while ((int)(now - tnext) < 0 && (isk | isv)); \
      tnext = ((int)(now - tnext) > PACE ? now - PACE : tnext) + PACE;     \
      LOADT(R, rr, so); \
      SB(); \
      tR = isk ? 0 : isv ? 1 : 2; \
      if (isk) { ++nk; idk = nk < NS ? idk + NW : -1; \
        if (nk + DEMAND > NS) *(volatile LAS u32*)(L + L_NEED + wave * 4) = (u32)(nk + DEMAND - NS); } \
      nv += isv; done = ((isk | isv) == 0u && idk >= NTILES) ? done + 1 : 0; \
      if (nk >= NS - 14) { \
        if (isv && phase == 1u && nk <= NS && nv > NS - 24) *(volatile LAS u32*)(L + L_PRE + wave * 4) = (u32)max(AHEAD - (NS - nk), 0) + 1u;     \
        if (idk < 0) vq = *(volatile LAS u32*)(L + L_Q + (nk - NS) * 4); \
        vn = *(volatile LAS u32*)(L + L_NPRE); vv = *(volatile LAS u32*)(L + L_Q + max(nv - NS, 0) * 4); } }
    STEP(A, tA) STEP(B, tB)
#pragma unroll
    for (int pf = 0; pf < NPK - 2; ++pf) QKTILE(E[pf])
    do { STEP(A, tA) STEP(B, tB) } while (done < 2);
    WAIT_VM0();
    ROLES()
    {
      const float M = x32_max(x16_max(m));
      const float sc = __builtin_amdgcn_exp2f(m - M);
      l = wave_sum(l * sc) * 0.25f;
      float a[8];
#pragma unroll
      for (int k = 0; k < 8; ++k) a[k] = ac[k >> 1][k & 1] * sc;
#pragma unroll
      for (int k = 0; k < 4; ++k) a[k] = fold32(a[k], a[4 + k]);
#pragma unroll
      for (int k = 0; k < 2; ++k) a[k] = fold16(a[k], a[2 + k]);
      LAS float* rec = (LAS float*)(L + L_MRG + wave * 528);
      *(LAS f32x2*)(rec + 4 + 8 * c + 4 * (lane >> 5) + 2 * (r & 1)) = f32x2{a[0], a[1]};
      if (lane == 0) *(LAS f32x2*)rec = f32x2{M, l};
    }
    LDS_BARRIER();
    if (tid < 128) {
      const LAS float* rec = (const LAS float*)(L + L_MRG);
      float M = rec[0];
#pragma unroll
      for (int w = 1; w < NW; ++w) M = fmaxf(M, rec[w * 132]);
      float Ls = 0.f, ov = 0.f;
#pragma unroll
      for (int w = 0; w < NW; ++w) { const float wt = __builtin_amdgcn_exp2f(rec[w * 132] - M); Ls = fmaf(wt, rec[w * 132 + 1], Ls); ov = fmaf(wt, rec[w * 132 + 4 + tid], ov); }
      __builtin_amdgcn_raw_buffer_store_b128(u32x4{fbits(ov), tag, fbits((tid & 1) ? Ls : M), tag}, rw, WS_OP + ((h * 8 + o) * 128 + tid) * 16, 0, AUX_SC1);
    }
  } else {
    xres = 0;
    ROLES()
    tag = __builtin_amdgcn_readfirstlane(e0) + 1u;
    asm volatile("" :: "s"(tag));
    if (sp == 0 && lane == 0) __hip_atomic_store(cnt, (u64)tag << 32, RLX_AGENT);
    if (lane < 9) *(volatile LAS u32*)(L + L_NEED + lane * 4) = 0u;
    for (int i = lane; i < NCH + 8; i += 64) *(volatile LAS u32*)(L + L_Q + i * 4) = Q_EMPTY;
    LDS_BARRIER();
    {
      u32x4 v[8];
      __builtin_amdgcn_s_sleep(16);
      sweep2<8>(rw, WS_QP + (h * 8 * 256 + b * 128 + 2 * lane) * 8, 2048, tag, v, 0);
      float a0 = 0.f, a1 = 0.f;
#pragma unroll
      for (int k = 0; k < 8; ++k) { a0 += bitsf(v[k][0]); a1 += bitsf(v[k][2]); }
      *(LAS u32*)(L + L_QBF + lane * 4) = pack2(a0, a1);
    }
    LDS_BARRIER();
    const bool ffn = fh >= 0;
    f32x4 yv[3];
    if (woj >= 0 && lane < 2) {
      const u32 t = __builtin_nontemporal_load((const u32*)(wo + (size_t)(h * 128 + lane * 127) * (DM * 2) + woj * 256)); asm volatile("" :: "v"(t)); }
    if (ffn) {
      {
        u32 xr[2][5], xs[2][3];
#pragma unroll
        for (int bb = 0; bb < 2; ++bb) {
#pragma unroll
          for (int j = 0; j < 5; ++j) xr[bb][j] = x32[bb * 320 + lane + 64 * j];
#pragma unroll
          for (int j = 0; j < 3; ++j) xs[bb][j] = x32[bb * 320 + 160 * fh + min(lane + 64 * j, 159)];
        }
        const LnStats st = ln_stats(xr);
        LAS f32x2* xn = (LAS f32x2*)(L + L_XN);
#pragma unroll
        for (int j = 0; j < 3; ++j) if (lane + 64 * j < 160) {
          xn[2 * (lane + 64 * j)] = f32x2{ln_apply(bflo(xs[0][j]), st.mb[0], st.den[0], st.rcp[0]), ln_apply(bflo(xs[1][j]), st.mb[1], st.den[1], st.rcp[1])};
          xn[2 * (lane + 64 * j) + 1] = f32x2{ln_apply(bfhi(xs[0][j]), st.mb[0], st.den[0], st.rcp[0]), ln_apply(bfhi(xs[1][j]), st.mb[1], st.den[1], st.rcp[1])};
        }
        asm volatile("s_waitcnt lgkmcnt(0)" ::: "memory");
      }
      float ga[16];
#pragma unroll
      for (int k = 0; k < 16; ++k) ga[k] = 0.f;
      const char* gp = (c < 8 ? wg : wu) + (size_t)(320 * fh + r) * (DF * 2) + h * 128 + (c & 7) * 16;
      for (int ch = 0; ch < 5; ++ch) {
        u32x4 gv[16];
#pragma unroll
        for (int i = 0; i < 16; ++i) gv[i] = __builtin_nontemporal_load((const u32x4*)(gp + (size_t)(ch * 16 + i) * 4 * (DF * 2)));
#pragma unroll
        for (int i = 0; i < 16; ++i) {
          const f32x2 xv = ((const LAS f32x2*)(L + L_XN))[64 * ch + 4 * i + r];
#pragma unroll
          for (int k = 0; k < 4; ++k) {
            const float w0 = bflo(gv[i][k]), w1 = bfhi(gv[i][k]);
            ga[2 * k] = fmaf(xv[0], w0, ga[2 * k]); ga[2 * k + 1] = fmaf(xv[0], w1, ga[2 * k + 1]);
            ga[8 + 2 * k] = fmaf(xv[1], w0, ga[8 + 2 * k]); ga[8 + 2 * k + 1] = fmaf(xv[1], w1, ga[8 + 2 * k + 1]);
          }
        }
      }
#pragma unroll
      for (int k = 0; k < 8; ++k) ga[k] = fold32(ga[k], ga[8 + k]);
#pragma unroll
      for (int k = 0; k < 4; ++k) ga[k] = fold16(ga[k], ga[4 + k]);
      LAS float* gt = (LAS float*)(L + L_GT);
#pragma unroll
      for (int k = 0; k < 4; ++k) gt[(lane >> 5) * 128 + (8 * (c & 7) + 4 * (r & 1) + k) * 2 + (c >> 3)] = ga[k];
      asm volatile("s_waitcnt lgkmcnt(0)" ::: "memory");
      f32x2 gu[2];
#pragma unroll
      for (int bb = 0; bb < 2; ++bb) {
        gu[bb] = *(const LAS f32x2*)(gt + bb * 128 + lane * 2);
        __builtin_amdgcn_raw_buffer_store_b128(u32x4{fbits(gu[bb][0]), tag, fbits(gu[bb][1]), tag}, rw, WS_GP + (((h * 2 + fh) * 2 + bb) * 64 + lane) * 16, 0, AUX_SC1);
      }
      for (int sl = 0; sl < wdn; ++sl) {
#pragma unroll
        for (int j = 0; j < 16; ++j)
          __builtin_amdgcn_global_load_lds((const GAS u32*)(wd + (size_t)(h * 64 + 4 * j + r) * (DM * 2) + (wd0 + sl) * 256 + c * 16), (LAS u32*)(L + L_W + sl * 16384 + j * 1024), 16, 0, AUX_NT);
      }
      __builtin_amdgcn_s_sleep(127);
      u32x4 v[2];
      sweep2<2>(rw, WS_GP + ((h * 2 + 1 - fh) * 128 + lane) * 16, 1024, tag, v, 1);
      float hv[2];
#pragma unroll
      for (int bb = 0; bb < 2; ++bb) {
        const float g = rbf(gu[bb][0] + bitsf(v[bb][0])), u = rbf(gu[bb][1] + bitsf(v[bb][2]));
        const float z = 0.7978845608028654f * (g + 0.044715f * g * g * g);
        const float th = 1.f - 2.f / (__expf(2.f * z) + 1.f);
        hv[bb] = rbf(rbf(0.5f * g * (1.f + th)) * u);
      }
      ((LAS f32x2*)(L + L_HS))[lane] = f32x2{hv[0], hv[1]};
      __builtin_amdgcn_s_waitcnt(0x0070);
#pragma unroll
      for (int sl = 0; sl < 3; ++sl) {
        float y00 = 0.f, y01 = 0.f, y10 = 0.f, y11 = 0.f;
        if (sl < wdn) {
#pragma unroll 8
          for (int f = 0; f < 64; ++f) {
            const float w0 = bf2f(*(const LAS u16*)(L + L_W + sl * 16384 + f * 256 + lane * 2)), w1 = bf2f(*(const LAS u16*)(L + L_W + sl * 16384 + f * 256 + 128 + lane * 2));
            const f32x2 hh = ((const LAS f32x2*)(L + L_HS))[f];
            y00 = fmaf(hh[0], w0, y00); y10 = fmaf(hh[1], w0, y10); y01 = fmaf(hh[0], w1, y01); y11 = fmaf(hh[1], w1, y11);
          }
        }
        yv[sl] = f32x4{y00, y10, y01, y11};
      }
    }
    {
      u32 filled = 0, last = 0, npre = 0;
      for (u32 spins = 0; spins < (SPIN_CAP << 6); ++spins) {
        const u32x4 nd = *(volatile LAS u32x4*)(L + L_NEED);
        const u32 need = __builtin_amdgcn_readfirstlane(max(max(nd[0], nd[1]), max(nd[2], nd[3])));
        const u32x4 pd = *(volatile LAS u32x4*)(L + L_PRE);
        const u32 mn = __builtin_amdgcn_readfirstlane(min(min(pd[0], pd[1]), min(pd[2], pd[3])));
        if (mn > npre + 1u) { npre = mn - 1u; *(volatile LAS u32*)(L + L_NPRE) = npre; }
        if (filled >= npre && filled + (last + LATE >= (u32)NCH ? 1u : 0u) >= need) { __builtin_amdgcn_s_sleep(1); continue; }
        u32 got = NCH;
        for (u32 sp2 = 0; sp2 < SPIN_CAP; ++sp2) {
          u64 old = 0;
          if (lane == 0) old = __hip_atomic_fetch_add(cnt, 1ull, RLX_AGENT);
          if ((u32)__builtin_amdgcn_readfirstlane((u32)(old >> 32)) == tag) { got = __builtin_amdgcn_readfirstlane((u32)old); break; }
        }
        if (got >= (u32)NCH) break;
        last = got;
        *(volatile LAS u32*)(L + L_Q + filled * 4) = POOL0 + got * NW;
        ++filled;
      }
      for (int i = filled + lane; i < NCH + 8; i += 64) *(volatile LAS u32*)(L + L_Q + i * 4) = NTILES;
    }
    if (woj >= 0) {
#pragma unroll
      for (int j = 0; j < 32; ++j)
        __builtin_amdgcn_global_load_lds((const GAS u32*)(wo + (size_t)(h * 128 + 4 * j + r) * (DM * 2) + woj * 256 + ((((c >> 1) ^ (r | ((j >> 1) & 1) << 2)) << 1) | (c & 1)) * 16),
                                         (LAS u32*)(L + L_W + j * 1024), 16, 0, AUX_NT);
    }
    WAIT_VM0();
    LDS_BARRIER();
    if (ffn) {
#pragma unroll
      for (int sl = 0; sl < 3; ++sl) if (sl < wdn) {
        const int yo = WS_YP + (h * 640 + (wd0 + sl) * 128 + lane) * 16;
        __builtin_amdgcn_raw_buffer_store_b128(u32x4{fbits(yv[sl][0]), tag, fbits(yv[sl][1]), tag}, rw, yo, 0, AUX_SC1);
        __builtin_amdgcn_raw_buffer_store_b128(u32x4{fbits(yv[sl][2]), tag, fbits(yv[sl][3]), tag}, rw, yo + 1024, 0, AUX_SC1);
      }
    }
    WAIT_VM0();
  }
  if (woj >= 0) {
    s16x8 bw[2][4];
    if (wave < NW) {
      const int g = lane >> 4, q = (lane & 15) >> 2, p = lane & 3;
#pragma unroll
      for (int t = 0; t < 2; ++t)
#pragma unroll
        for (int s = 0; s < 4; ++s) {
          const LAS char* a0 = L + L_W + (32 * s + 8 * g + q) * 256 + ((2 * wave + t) ^ (q | (g & 1) << 2)) * 32 + 8 * p;
          const s16x4 lo = __builtin_bit_cast(s16x4, __builtin_amdgcn_ds_read_tr16_b64_v4i16((LAS s16x4*)a0));
          const s16x4 hi = __builtin_bit_cast(s16x4, __builtin_amdgcn_ds_read_tr16_b64_v4i16((LAS s16x4*)(a0 + 4 * 256)));
          bw[t][s] = s16x8{lo[0], lo[1], lo[2], lo[3], hi[0], hi[1], hi[2], hi[3]};
        }
      const int bb = tid >> 7, d = tid & 127;
      u32x4 v[4];
      sweep2<4>(rw, WS_OP + ((h * 8 + bb * 4) * 128 + d) * 16, 2048, tag, v, 0);
      float Mv[4], Lv[4];
#pragma unroll
      for (int s = 0; s < 4; ++s) { const float ml = bitsf(v[s][2]), ot = dpp<XOR1>(ml); Mv[s] = (lane & 1) ? ot : ml; Lv[s] = (lane & 1) ? ml : ot; }
      const float M = fmaxf(fmaxf(Mv[0], Mv[1]), fmaxf(Mv[2], Mv[3]));
      float Ls = 0.f, a = 0.f;
#pragma unroll
      for (int s = 0; s < 4; ++s) { const float wt = __builtin_amdgcn_exp2f(Mv[s] - M); Ls = fmaf(wt, Lv[s], Ls); a = fmaf(wt, bitsf(v[s][0]), a); }
      *(LAS u16*)(L + L_OF + bb * 256 + d * 2) = f2bf(fdiv(a, Ls, __builtin_amdgcn_rcpf(Ls)));
    }
    LDS_BARRIER();
    if (wave < NW) {
      f32x4 acc0 = {0.f, 0.f, 0.f, 0.f}, acc1 = {0.f, 0.f, 0.f, 0.f};
#pragma unroll
      for (int s = 0; s < 4; ++s) {
        const bf16x8 a = __builtin_bit_cast(bf16x8, *(const LAS u32x4*)(L + L_OF + (lane & 1) * 256 + (32 * s + 8 * (lane >> 4)) * 2));
        acc0 = __builtin_amdgcn_mfma_f32_16x16x32_bf16(a, __builtin_bit_cast(bf16x8, bw[0][s]), acc0, 0, 0, 0);
        acc1 = __builtin_amdgcn_mfma_f32_16x16x32_bf16(a, __builtin_bit_cast(bf16x8, bw[1][s]), acc1, 0, 0, 0);
      }
      if (lane < 32) __builtin_amdgcn_raw_buffer_store_b128(u32x4{fbits(lane < 16 ? acc0[0] : acc1[0]), tag, fbits(lane < 16 ? acc0[1] : acc1[1]), tag}, rw,
                                                            WS_SP + (h * 640 + woj * 128 + 32 * wave + lane) * 16, 0, AUX_SC1);
    }
  }
  if (wave < 3) {
    const int hp = lane & 31;
    int os = WS_SP + (hp * 1280 + jf) * 8, oy = WS_YP + (min(hp, NCB - 1) * 1280 + jf) * 8;
    u32x2 gs = __builtin_amdgcn_raw_buffer_load_b64(rw, os, 0, AUX_SC1), gy = __builtin_amdgcn_raw_buffer_load_b64(rw, oy, 0, AUX_SC1);
    for (u32 spins = 0; spins < SPIN_CAP; ++spins) {
      if (__all(!actf || ((gs[1] == tag) & (gy[1] == tag)))) break;
      asm volatile("" : "+v"(os), "+v"(oy) :: "memory");
      gs = __builtin_amdgcn_raw_buffer_load_b64(rw, os, 0, AUX_SC1); gy = __builtin_amdgcn_raw_buffer_load_b64(rw, oy, 0, AUX_SC1);
    }
    const float sv = actf ? bitsf(gs[0]) : 0.f, yy = (actf && hp < NCB) ? bitsf(gy[0]) : 0.f;
    const float s = rbf(half_sum(sv)), y = rbf(half_sum(yy));
    const float gate = rbf(y / (1.f + __expf(-y)));
    if (actf && hp == 0) out[bf * DM + colf] = f2bf(bf2f(xres) + rbf(s * gate));
    if (blk == 0 && tid == 0) __hip_atomic_store(epoch, tag, RLX_AGENT);
  }
}

extern "C" void kernel_launch(void* const* d_in, const int* in_sizes, int n_in, void* d_out, int out_size, void* d_ws, size_t ws_size, hipStream_t stream) {
  k_fused<<<256, NTHR, 0, stream>>>((const u16*)d_in[0], (const char*)d_in[1], (const char*)d_in[2], (const char*)d_in[3], (const char*)d_in[4],
                                    (const char*)d_in[6], (const char*)d_in[7], (const char*)d_in[8], (u16*)d_out, (char*)d_ws);
}
```
